```python
import torch

def _softmax(x, axis=-1):
    return torch.softmax(x, dim=axis)
def _gelu(x):
    return torch.nn.functional.gelu(x, approximate="tanh")
def _silu(x):
    return torch.nn.functional.silu(x)
def _sigmoid(x):
    return torch.sigmoid(x)
def _layernorm(x, axis=-1, eps=1e-5):
    m = x.mean(dim=axis, keepdim=True)
    v = x.var(dim=axis, keepdim=True, unbiased=False)
    return (x - m) / torch.sqrt(v + eps)
def _scatter_add(tgt, idx, src, axis=0):
    return tgt.clone().index_add_(axis, idx, src)
def _segment_sum(src, seg, n):
    out = torch.zeros((n,) + src.shape[1:], dtype=src.dtype)
    return out.index_add_(0, seg, src)
def _pad(x, pw):
    flat = tuple(v for pair in reversed(pw) for v in pair)
    return torch.nn.functional.pad(x, flat)

kernel_name = 'gated_mix__softmax_full_mha__dense_activation_relu__prompt'

def setup_inputs():

    _w = (lambda a: (lambda t: t.to(torch.bfloat16) if t.is_floating_point() else t)(torch.as_tensor(a)))
    def _A_setup():
        B, S, D = 1, 2048, 512
        return {
            "x": _w((lambda *s: torch.randn(*s, dtype=torch.bfloat16) * 0.05)(B, S, D)),
            "wqkv": _w((lambda *s: torch.randn(*s, dtype=torch.bfloat16) * 0.8)(D, 3 * D)),
            "wo": _w((lambda *s: torch.randn(*s, dtype=torch.bfloat16) * 0.1)(D, D)),
        }
    _A_in = _A_setup()

    def _B_setup():
        B, S, D, Dff = 1, 2048, 512, 1376
        return {
            "x": _w((lambda *s: torch.randn(*s, dtype=torch.bfloat16) * 0.05)(B, S, D)),
            "w1": _w((lambda *s: torch.randn(*s, dtype=torch.bfloat16) * 0.05)(D, Dff)),
            "w2": _w((lambda *s: torch.randn(*s, dtype=torch.bfloat16) * 0.05)(Dff, D)),
        }
    _B_in = _B_setup()

    return {
        "A_x": _A_in["x"],
        "A_wqkv": _A_in["wqkv"],
        "A_wo": _A_in["wo"],
        "B_x": _B_in["x"],
        "B_w1": _B_in["w1"],
        "B_w2": _B_in["w2"],
        "gate_w": _w((lambda *s: torch.randn(*s, dtype=torch.bfloat16) * 0.05)(512) * 16.0),
    }

def reference(A_x, A_wqkv, A_wo, B_x, B_w1, B_w2, gate_w):
    def _A_ref(x, wqkv, wo):
        B, S, D = x.shape
        H = 32; Dh = D // H
        qkv = (lambda x, *d: x.permute(*d))((x @ wqkv).reshape(B, S, 3, H, Dh), 2, 0, 3, 1, 4)
        q, k, v = qkv[0], qkv[1], qkv[2]
        scale = Dh ** -0.5
        scores = torch.einsum("bhqd,bhkd->bhqk", q, k) * scale
        p = _softmax(scores, axis=-1)
        o = torch.einsum("bhqk,bhkd->bhqd", p, v)
        return (lambda x, *d: x.permute(*d))(o, 0, 2, 1, 3).reshape(B, S, D) @ wo

    def _B_ref(x, w1, w2):
        h = torch.einsum("bsd,df->bsf", x, w1)
        zero = torch.zeros_like(h)
        h = torch.maximum(h, zero)
        return torch.einsum("bsf,fd->bsd", h, w2)

    x = A_x
    g = _sigmoid((x * gate_w).sum(-1, keepdims=True))
    a = _A_ref(x, A_wqkv, A_wo)
    b = _B_ref(x, B_w1, B_w2)
    return g * a + (1 - g) * b
```

```cpp
#include <hip/hip_runtime.h>
#include <hip/hip_bf16.h>
#include <stdint.h>

typedef __attribute__((ext_vector_type(8))) short bf16x8;
typedef __attribute__((ext_vector_type(4))) short s16x4;
typedef __attribute__((ext_vector_type(16))) float f32x16;
typedef __attribute__((ext_vector_type(4))) unsigned u32x4;
typedef __attribute__((ext_vector_type(2))) unsigned u32x2;
typedef unsigned short u16;

constexpr int SEQ = 2048, DM = 512, NH = 32, DH = 16, DFF = 1376;
constexpr float QSCALE = 0.25f * 1.4426950408889634f;
constexpr float RESC_THR = 8.0f;

#define MFMA32(a, b, c) __builtin_amdgcn_mfma_f32_32x32x16_bf16(a, b, c, 0, 0, 0)

__device__ __forceinline__ unsigned cvtpk(float lo, float hi) {
    typedef __bf16 b2 __attribute__((ext_vector_type(2))); typedef float f2 __attribute__((ext_vector_type(2)));
    f2 v = {lo, hi}; return __builtin_bit_cast(unsigned, __builtin_convertvector(v, b2));
}
__device__ __forceinline__ float bf2f(u16 v) { return __uint_as_float(((unsigned)v) << 16); }
__device__ __forceinline__ float rbf(float x) { return bf2f((u16)(cvtpk(x, x) & 0xffffu)); }

typedef __attribute__((address_space(3))) const char* lds_cptr;
typedef short v4i16_t __attribute__((ext_vector_type(4)));
__device__ __forceinline__ s16x4 ldtr(const char* p) {
    return __builtin_bit_cast(s16x4, __builtin_amdgcn_ds_read_tr16_b64_v4i16((__attribute__((address_space(3))) v4i16_t*)p));
}

__device__ __forceinline__ unsigned offA(unsigned row, unsigned ch) { return row * 128u + ((ch ^ (row & 7u)) << 4); }
template <int BN> __device__ __forceinline__ unsigned offB(unsigned row, unsigned ch) {
    if (BN == 128) return 256u * row + 16u * (ch ^ (((row & 3u) << 2) | ((row >> 2) & 3u)));
    else return 128u * row + 16u * (ch ^ (((row >> 1) & 1u) << 2));
}
template <int BN> __device__ __forceinline__ unsigned trAddr(unsigned lane, unsigned c, unsigned ks, unsigned t) {
    const unsigned h = lane >> 5, blk = (lane >> 4) & 1, q = (lane & 15) >> 2, p = lane & 3;
    return offB<BN>(16 * ks + 8 * h + 4 * t + q, 4 * c + 2 * blk + (p >> 1)) + 8 * (p & 1);
}

template <int BM, int BN, int WM, int WN, bool SWAP>
__device__ __forceinline__ void gemm_loop(const u16* __restrict__ A, int lda, const u16* __restrict__ B, int ldb, int K,
                                          int m0, int n0, int nvalid, char* ldsA, char* ldsB,
                                          f32x16 (&acc)[BM / WM / 32][BN / WN / 32]) {
    constexpr int MI = BM / WM / 32, NI = BN / WN / 32;
    constexpr int ACH = BM * 8 / 256, BCH = 64 * (BN / 8) / 256;
    const int tid = threadIdx.x, lane = tid & 63, wid = tid >> 6;
    const int wm = wid / WN, wn = wid % WN;
    const int nk = (K + 63) / 64;
    u32x4 ra[ACH], rb[BCH];
    auto gload = [&](int kt) {
        const int k0 = kt * 64;
#pragma unroll
        for (int i = 0; i < ACH; ++i) {
            const int c = tid + 256 * i, row = c >> 3, ch = c & 7;
            const int k = k0 + ch * 8;
            ra[i] = (k < K) ? *(const u32x4*)(A + (size_t)(m0 + row) * lda + k) : u32x4{0, 0, 0, 0};
        }
#pragma unroll
        for (int i = 0; i < BCH; ++i) {
            const int c = tid + 256 * i, row = c / (BN / 8), ch = c % (BN / 8);
            const int k = k0 + row, n = n0 + ch * 8;
            rb[i] = (k < K && n < nvalid) ? *(const u32x4*)(B + (size_t)k * ldb + n) : u32x4{0, 0, 0, 0};
        }
    };
    auto lstore = [&]() {
#pragma unroll
        for (int i = 0; i < ACH; ++i) { const int c = tid + 256 * i, row = c >> 3, ch = c & 7; *(u32x4*)(ldsA + offA(row, ch)) = ra[i]; }
#pragma unroll
        for (int i = 0; i < BCH; ++i) { const int c = tid + 256 * i, row = c / (BN / 8), ch = c % (BN / 8); *(u32x4*)(ldsB + offB<BN>(row, ch)) = rb[i]; }
    };
    gload(0);
    for (int kt = 0; kt < nk; ++kt) {
        __syncthreads();
        lstore();
        __syncthreads();
        if (kt + 1 < nk) gload(kt + 1);
#pragma unroll
        for (int ks = 0; ks < 4; ++ks) {
            bf16x8 af[MI], bfr[NI];
#pragma unroll
            for (int mi = 0; mi < MI; ++mi) {
                const unsigned row = wm * (BM / WM) + mi * 32 + (lane & 31);
                af[mi] = *(const bf16x8*)(ldsA + offA(row, 2 * ks + (lane >> 5)));
            }
#pragma unroll
            for (int ni = 0; ni < NI; ++ni) {
                const unsigned c = wn * (BN / WN / 32) + ni;
                const s16x4 lo = ldtr(ldsB + trAddr<BN>(lane, c, ks, 0));
                const s16x4 hi = ldtr(ldsB + trAddr<BN>(lane, c, ks, 1));
                bfr[ni] = __builtin_shufflevector(lo, hi, 0, 1, 2, 3, 4, 5, 6, 7);
            }
#pragma unroll
            for (int mi = 0; mi < MI; ++mi)
#pragma unroll
                for (int ni = 0; ni < NI; ++ni)
                    acc[mi][ni] = SWAP ? MFMA32(bfr[ni], af[mi], acc[mi][ni]) : MFMA32(af[mi], bfr[ni], acc[mi][ni]);
        }
    }
}

struct P1 {
    const u16* x; const u16* wqkv; const u16* w1; const u16* gate_w;
    u16* Q; u16* Kimg; u16* Vimg; u16* Hb; float* G;
};

constexpr int K1_COLT = 12 + 11;
constexpr int K1_GEMM_BLOCKS = 16 * K1_COLT;
constexpr int K1_GATE_BLOCKS = 64;

__global__ void __launch_bounds__(256, 2) k1_proj(P1 p) {
    __shared__ __attribute__((aligned(16))) char lds[32768];
    const int b = blockIdx.x, tid = threadIdx.x, lane = tid & 63, wid = tid >> 6;
    if (b >= K1_GEMM_BLOCKS) {
        const int gw = (b - K1_GEMM_BLOCKS) * 4 + wid;
        const u32x4 gv = *(const u32x4*)(p.gate_w + lane * 8);
        for (int r = 0; r < 8; ++r) {
            const int row = gw * 8 + r;
            const u32x4 xv = *(const u32x4*)(p.x + (size_t)row * DM + lane * 8);
            float s = 0.f;
#pragma unroll
            for (int j = 0; j < 4; ++j) {
                s += rbf(bf2f((u16)(xv[j] & 0xffff)) * bf2f((u16)(gv[j] & 0xffff)));
                s += rbf(bf2f((u16)(xv[j] >> 16)) * bf2f((u16)(gv[j] >> 16)));
            }
#pragma unroll
            for (int o = 32; o > 0; o >>= 1) s += __shfl_xor(s, o);
            if (lane == 0) { const float z = rbf(s); p.G[row] = rbf(1.0f / (1.0f + __expf(-z))); }
        }
        return;
    }
    const int colt = b % K1_COLT, rowt = b / K1_COLT;
    const int m0 = rowt * 128;
    char* ldsA = lds; char* ldsB = lds + 16384;
    f32x16 acc[2][2];
#pragma unroll
    for (int i = 0; i < 2; ++i)
#pragma unroll
        for (int j = 0; j < 2; ++j) acc[i][j] = f32x16{};
    const int wm = wid >> 1, wn = wid & 1, h = lane >> 5, l31 = lane & 31;
    if (colt >= 8 && colt < 12) {
        const int n0 = colt * 128;
        gemm_loop<128, 128, 2, 2, false>(p.x, DM, p.wqkv, 3 * DM, DM, m0, n0, 3 * DM, ldsA, ldsB, acc);
#pragma unroll
        for (int mi = 0; mi < 2; ++mi)
#pragma unroll
            for (int ni = 0; ni < 2; ++ni) {
                const int n = n0 - 1024 + wn * 64 + ni * 32 + l31;
                const int head = n >> 4, d = n & 15;
                const int t = (m0 + wm * 64 + mi * 32) >> 5;
#pragma unroll
                for (int s = 0; s < 2; ++s) {
                    u32x4 v;
#pragma unroll
                    for (int j = 0; j < 4; ++j) v[j] = cvtpk(acc[mi][ni][8 * s + 2 * j], acc[mi][ni][8 * s + 2 * j + 1]);
                    *(u32x4*)(p.Vimg + (size_t)head * 32768 + t * 512 + s * 256 + h * 128 + d * 8) = v;
                }
            }
    } else {
        const bool isf = colt >= 12;
        const int n0 = isf ? (colt - 12) * 128 : colt * 128;
        if (isf) gemm_loop<128, 128, 2, 2, true>(p.x, DM, p.w1, DFF, DM, m0, n0, DFF, ldsA, ldsB, acc);
        else     gemm_loop<128, 128, 2, 2, true>(p.x, DM, p.wqkv, 3 * DM, DM, m0, n0, 3 * DM, ldsA, ldsB, acc);
#pragma unroll
        for (int mi = 0; mi < 2; ++mi)
#pragma unroll
            for (int ni = 0; ni < 2; ++ni) {
                const int tok = m0 + wm * 64 + mi * 32 + l31;
#pragma unroll
                for (int g = 0; g < 4; ++g) {
                    const int n = n0 + wn * 64 + ni * 32 + 8 * g + 4 * h;
                    float v0 = acc[mi][ni][4 * g], v1 = acc[mi][ni][4 * g + 1], v2 = acc[mi][ni][4 * g + 2], v3 = acc[mi][ni][4 * g + 3];
                    if (isf) {
                        if (n < DFF) {
                            u32x2 o; o[0] = cvtpk(fmaxf(v0, 0.f), fmaxf(v1, 0.f)); o[1] = cvtpk(fmaxf(v2, 0.f), fmaxf(v3, 0.f));
                            *(u32x2*)(p.Hb + (size_t)tok * DFF + n) = o;
                        }
                    } else if (colt < 4) {
                        u32x2 o; o[0] = cvtpk(v0 * QSCALE, v1 * QSCALE); o[1] = cvtpk(v2 * QSCALE, v3 * QSCALE);
                        *(u32x2*)(p.Q + ((size_t)(n >> 4) * SEQ + tok) * 16 + (n & 15)) = o;
                    } else {
                        const int nn = n - 512;
                        u32x2 o; o[0] = cvtpk(v0, v1); o[1] = cvtpk(v2, v3);
                        *(u32x2*)(p.Kimg + ((size_t)(nn >> 4) * SEQ + tok) * 16 + (nn & 15)) = o;
                    }
                }
            }
    }
}

struct P2 { const u16* Q; const u16* Kimg; const u16* Vimg; u16* O; };
constexpr int A_LDS_K = 0, A_LDS_V = 65536, A_LDS_C = 131072, A_LDS_BYTES = 131072 + 1024;

__global__ void __launch_bounds__(512, 2) k2_attn(P2 p) {
    __shared__ __attribute__((aligned(16))) char lds[A_LDS_BYTES];
    const int b = blockIdx.x, tid = threadIdx.x, lane = tid & 63, wid = tid >> 6;
    const int xcd = b & 7, idx = b >> 3;
    const int head = xcd * 4 + (idx >> 3), qb = idx & 7;
    const int h = lane >> 5, l31 = lane & 31;
    {
        const u16* kg = p.Kimg + (size_t)head * 32768;
        const u16* vg = p.Vimg + (size_t)head * 32768;
#pragma unroll
        for (int i = 0; i < 8; ++i) {
            const int c = tid + 512 * i;
            *(u32x4*)(lds + A_LDS_K + c * 16) = *(const u32x4*)(kg + c * 8);
        }
#pragma unroll
        for (int i = 0; i < 8; ++i) {
            const int c = tid + 512 * i;
            *(u32x4*)(lds + A_LDS_V + c * 16) = *(const u32x4*)(vg + c * 8);
        }
        if (tid < 64) {
            const unsigned v = ((tid & 31) == 0) ? 0x3F803F80u : 0u;
            const u32x4 vv = {v, v, v, v};
            *(u32x4*)(lds + A_LDS_C + (tid & 31) * 16 + (tid >> 5) * 512) = vv;
        }
    }
    const int q0 = qb * 256 + wid * 32;
    const bf16x8 qf = *(const bf16x8*)(p.Q + ((size_t)head * SEQ + q0 + l31) * 16 + h * 8);
    __syncthreads();
    const char* kp = lds + A_LDS_K + l31 * 32 + h * 16;
    const char* vp = lds + ((l31 < 16) ? (A_LDS_V + h * 256 + l31 * 16) : (A_LDS_C + ((l31 == 16) ? 0 : 16)));
    const int vstep = (l31 < 16) ? 1024 : 0;
    f32x16 negm, o = f32x16{};
    {
        const bf16x8 kf = *(const bf16x8*)(kp);
        f32x16 s = MFMA32(kf, qf, f32x16{});
        float mx = s[0];
#pragma unroll
        for (int r = 1; r < 16; ++r) mx = fmaxf(mx, s[r]);
        mx = fmaxf(mx, __shfl_xor(mx, 32));
#pragma unroll
        for (int r = 0; r < 16; ++r) negm[r] = -mx;
    }
    for (int t = 0; t < 64; ++t) {
        const bf16x8 kf = *(const bf16x8*)(kp + t * 1024);
        const bf16x8 v0 = *(const bf16x8*)(vp);
        const bf16x8 v1 = *(const bf16x8*)(vp + 512);
        vp += vstep;
        f32x16 s = MFMA32(kf, qf, negm);
        float mx = fmaxf(s[0], s[1]);
#pragma unroll
        for (int r = 2; r < 16; r += 2) mx = fmaxf(fmaxf(mx, s[r]), s[r + 1]);
        if (__any(mx > RESC_THR)) {
            mx = fmaxf(mx, __shfl_xor(mx, 32));
            const float dl = fmaxf(mx, 0.f);
            const float al = __builtin_amdgcn_exp2f(-dl);
#pragma unroll
            for (int r = 0; r < 16; ++r) { negm[r] -= dl; s[r] -= dl; o[r] *= al; }
        }
        u32x4 p0, p1;
#pragma unroll
        for (int j = 0; j < 4; ++j) {
            p0[j] = cvtpk(__builtin_amdgcn_exp2f(s[2 * j]), __builtin_amdgcn_exp2f(s[2 * j + 1]));
            p1[j] = cvtpk(__builtin_amdgcn_exp2f(s[8 + 2 * j]), __builtin_amdgcn_exp2f(s[8 + 2 * j + 1]));
        }
        o = MFMA32(v0, __builtin_bit_cast(bf16x8, p0), o);
        o = MFMA32(v1, __builtin_bit_cast(bf16x8, p1), o);
    }
    const float lsum = __shfl(o[8], l31);
    const float inv = 1.0f / lsum;
    u16* op = p.O + (size_t)(q0 + l31) * DM + head * 16 + 4 * h;
    u32x2 w0, w1;
    w0[0] = cvtpk(o[0] * inv, o[1] * inv); w0[1] = cvtpk(o[2] * inv, o[3] * inv);
    w1[0] = cvtpk(o[4] * inv, o[5] * inv); w1[1] = cvtpk(o[6] * inv, o[7] * inv);
    *(u32x2*)(op) = w0;
    *(u32x2*)(op + 8) = w1;
}

struct P3 { const u16* O; const u16* wo; const u16* Hb; const u16* w2; const float* G; u16* out; };

__global__ void __launch_bounds__(256, 2) k3_out(P3 p) {
    __shared__ __attribute__((aligned(16))) char lds[16384];
    const int b = blockIdx.x, tid = threadIdx.x, lane = tid & 63, wid = tid >> 6;
    const int colt = b & 7, rowt = b >> 3;
    const int m0 = rowt * 64, n0 = colt * 64;
    char* ldsA = lds; char* ldsB = lds + 8192;
    f32x16 acca[1][1], accb[1][1];
    acca[0][0] = f32x16{}; accb[0][0] = f32x16{};
    gemm_loop<64, 64, 2, 2, true>(p.O, DM, p.wo, DM, DM, m0, n0, DM, ldsA, ldsB, acca);
    gemm_loop<64, 64, 2, 2, true>(p.Hb, DFF, p.w2, DM, DFF, m0, n0, DM, ldsA, ldsB, accb);
    const int wm = wid >> 1, wn = wid & 1, h = lane >> 5, l31 = lane & 31;
    const int tok = m0 + wm * 32 + l31;
    const float g = p.G[tok];
    const float omg = rbf(1.0f - g);
#pragma unroll
    for (int gi = 0; gi < 4; ++gi) {
        const int n = n0 + wn * 32 + 8 * gi + 4 * h;
        float r[4];
#pragma unroll
        for (int j = 0; j < 4; ++j) {
            const float a = rbf(acca[0][0][4 * gi + j]), bb = rbf(accb[0][0][4 * gi + j]);
            r[j] = rbf(g * a) + rbf(omg * bb);
        }
        u32x2 o; o[0] = cvtpk(r[0], r[1]); o[1] = cvtpk(r[2], r[3]);
        *(u32x2*)(p.out + (size_t)tok * DM + n) = o;
    }
}

extern "C" void kernel_launch(void* const* d_in, const int* in_sizes, int n_in,
                              void* d_out, int out_size, void* d_ws, size_t ws_size,
                              hipStream_t stream) {
    const u16* x = (const u16*)d_in[0];
    const u16* wqkv = (const u16*)d_in[1];
    const u16* wo = (const u16*)d_in[2];
    const u16* w1 = (const u16*)d_in[4];
    const u16* w2 = (const u16*)d_in[5];
    const u16* gate_w = (const u16*)d_in[6];
    char* ws = (char*)d_ws;
    u16* Q = (u16*)(ws);
    u16* Kimg = (u16*)(ws + (2u << 20));
    u16* Vimg = (u16*)(ws + (4u << 20));
    u16* O = (u16*)(ws + (6u << 20));
    u16* Hb = (u16*)(ws + (8u << 20));
    float* G = (float*)(ws + (14u << 20));

    P1 p1{x, wqkv, w1, gate_w, Q, Kimg, Vimg, Hb, G};
    k1_proj<<<K1_GEMM_BLOCKS + K1_GATE_BLOCKS, 256, 0, stream>>>(p1);
    P2 p2{Q, Kimg, Vimg, O};
    k2_attn<<<256, 512, 0, stream>>>(p2);
    P3 p3{O, wo, Hb, w2, G, (u16*)d_out};
    k3_out<<<256, 256, 0, stream>>>(p3);
}
```

```cpp
#include <hip/hip_runtime.h>
#include <hip/hip_bf16.h>
#include <stdint.h>

typedef __attribute__((ext_vector_type(8))) short bf16x8;
typedef __attribute__((ext_vector_type(4))) short s16x4;
typedef __attribute__((ext_vector_type(16))) float f32x16;
typedef __attribute__((ext_vector_type(4))) unsigned u32x4;
typedef __attribute__((ext_vector_type(2))) unsigned u32x2;
typedef unsigned short u16;

constexpr int SEQ = 2048, DM = 512, NH = 32, DH = 16, DFF = 1376;
constexpr float QSCALE = 0.25f * 1.4426950408889634f;
constexpr float RESC_THR = 8.0f;

#define MFMA32(a, b, c) __builtin_amdgcn_mfma_f32_32x32x16_bf16(a, b, c, 0, 0, 0)

__device__ __forceinline__ unsigned cvtpk(float lo, float hi) {
    typedef __bf16 b2 __attribute__((ext_vector_type(2))); typedef float f2 __attribute__((ext_vector_type(2)));
    f2 v = {lo, hi}; return __builtin_bit_cast(unsigned, __builtin_convertvector(v, b2));
}
__device__ __forceinline__ float bf2f(u16 v) { return __uint_as_float(((unsigned)v) << 16); }
__device__ __forceinline__ float rbf(float x) { return bf2f((u16)(cvtpk(x, x) & 0xffffu)); }

typedef __attribute__((address_space(3))) const char* lds_cptr;
typedef short v4i16_t __attribute__((ext_vector_type(4)));
__device__ __forceinline__ s16x4 ldtr(const char* p) {
    return __builtin_bit_cast(s16x4, __builtin_amdgcn_ds_read_tr16_b64_v4i16((__attribute__((address_space(3))) v4i16_t*)p));
}

__device__ __forceinline__ unsigned offA(unsigned row, unsigned ch) { return row * 128u + ((ch ^ (row & 7u)) << 4); }
template <int BN> __device__ __forceinline__ unsigned offB(unsigned row, unsigned ch) {
    if (BN == 128) return 256u * row + 16u * (ch ^ (((row & 3u) << 2) | ((row >> 2) & 3u)));
    else return 128u * row + 16u * (ch ^ (((row >> 1) & 1u) << 2));
}
template <int BN> __device__ __forceinline__ unsigned trAddr(unsigned lane, unsigned c, unsigned ks, unsigned t) {
    const unsigned h = lane >> 5, blk = (lane >> 4) & 1, q = (lane & 15) >> 2, p = lane & 3;
    return offB<BN>(16 * ks + 8 * h + 4 * t + q, 4 * c + 2 * blk + (p >> 1)) + 8 * (p & 1);
}

template <int BM, int BN, int WM, int WN, bool SWAP, int G>
__device__ __forceinline__ void gemm_loop(const u16* __restrict__ A, int lda, const u16* __restrict__ B, int ldb, int K,
                                          int m0, int n0, int nvalid, char* ldsA, char* ldsB,
                                          f32x16 (&acc)[BM / WM / 32][BN / WN / 32]) {
    constexpr int MI = BM / WM / 32, NI = BN / WN / 32;
    constexpr int ACH = BM * 8 / 256, BCH = 64 * (BN / 8) / 256;
    const int tid = threadIdx.x, lane = tid & 63, wid = tid >> 6;
    const int wm = wid / WN, wn = wid % WN;
    const int nk = (K + 64 * G - 1) / (64 * G);
    constexpr int TS = BM * 128 + BN * 128;
    u32x4 ra[G][ACH], rb[G][BCH];
    auto gload = [&](int kt) {
#pragma unroll
        for (int gg = 0; gg < G; ++gg) {
        const int k0 = (kt * G + gg) * 64;
#pragma unroll
        for (int i = 0; i < ACH; ++i) {
            const int c = tid + 256 * i, row = c >> 3, ch = c & 7;
            const int k = k0 + ch * 8;
            ra[gg][i] = (k < K) ? *(const u32x4*)(A + (size_t)(m0 + row) * lda + k) : u32x4{0, 0, 0, 0};
        }
#pragma unroll
        for (int i = 0; i < BCH; ++i) {
            const int c = tid + 256 * i, row = c / (BN / 8), ch = c % (BN / 8);
            const int k = k0 + row, n = n0 + ch * 8;
            rb[gg][i] = (k < K && n < nvalid) ? *(const u32x4*)(B + (size_t)k * ldb + n) : u32x4{0, 0, 0, 0};
        }
        }
    };
    auto lstore = [&]() {
#pragma unroll
        for (int gg = 0; gg < G; ++gg) {
#pragma unroll
        for (int i = 0; i < ACH; ++i) { const int c = tid + 256 * i, row = c >> 3, ch = c & 7; *(u32x4*)(ldsA + gg * TS + offA(row, ch)) = ra[gg][i]; }
#pragma unroll
        for (int i = 0; i < BCH; ++i) { const int c = tid + 256 * i, row = c / (BN / 8), ch = c % (BN / 8); *(u32x4*)(ldsB + gg * TS + offB<BN>(row, ch)) = rb[gg][i]; }
        }
    };
    gload(0);
    for (int kt = 0; kt < nk; ++kt) {
        __syncthreads();
        lstore();
        __syncthreads();
        if (kt + 1 < nk) gload(kt + 1);
#pragma unroll
        for (int kk = 0; kk < 4 * G; ++kk) {
            const int ks = kk & 3, go = (kk >> 2) * TS;
            bf16x8 af[MI], bfr[NI];
#pragma unroll
            for (int mi = 0; mi < MI; ++mi) {
                const unsigned row = wm * (BM / WM) + mi * 32 + (lane & 31);
                af[mi] = *(const bf16x8*)(ldsA + go + offA(row, 2 * ks + (lane >> 5)));
            }
#pragma unroll
            for (int ni = 0; ni < NI; ++ni) {
                const unsigned c = wn * (BN / WN / 32) + ni;
                const s16x4 lo = ldtr(ldsB + go + trAddr<BN>(lane, c, ks, 0));
                const s16x4 hi = ldtr(ldsB + go + trAddr<BN>(lane, c, ks, 1));
                bfr[ni] = __builtin_shufflevector(lo, hi, 0, 1, 2, 3, 4, 5, 6, 7);
            }
#pragma unroll
            for (int mi = 0; mi < MI; ++mi)
#pragma unroll
                for (int ni = 0; ni < NI; ++ni)
                    acc[mi][ni] = SWAP ? MFMA32(bfr[ni], af[mi], acc[mi][ni]) : MFMA32(af[mi], bfr[ni], acc[mi][ni]);
        }
    }
}

struct P1 {
    const u16* x; const u16* wqkv; const u16* w1; const u16* gate_w;
    u16* Q; u16* Kimg; u16* Vimg; u16* Hb; float* G;
};

constexpr int K1_COLT = 12 + 11;
constexpr int K1_GEMM_BLOCKS = 16 * K1_COLT;
constexpr int K1_GATE_BLOCKS = 64;

__global__ void __launch_bounds__(256, 2) k1_proj(P1 p) {
    __shared__ __attribute__((aligned(16))) char lds[65536];
    const int b = blockIdx.x, tid = threadIdx.x, lane = tid & 63, wid = tid >> 6;
    if (b >= K1_GEMM_BLOCKS) {
        const int gw = (b - K1_GEMM_BLOCKS) * 4 + wid;
        const u32x4 gv = *(const u32x4*)(p.gate_w + lane * 8);
        for (int r = 0; r < 8; ++r) {
            const int row = gw * 8 + r;
            const u32x4 xv = *(const u32x4*)(p.x + (size_t)row * DM + lane * 8);
            float s = 0.f;
#pragma unroll
            for (int j = 0; j < 4; ++j) {
                s += rbf(bf2f((u16)(xv[j] & 0xffff)) * bf2f((u16)(gv[j] & 0xffff)));
                s += rbf(bf2f((u16)(xv[j] >> 16)) * bf2f((u16)(gv[j] >> 16)));
            }
#pragma unroll
            for (int o = 32; o > 0; o >>= 1) s += __shfl_xor(s, o);
            if (lane == 0) { const float z = rbf(s); p.G[row] = rbf(1.0f / (1.0f + __expf(-z))); }
        }
        return;
    }
    const int colt = b % K1_COLT, rowt = b / K1_COLT;
    const int m0 = rowt * 128;
    char* ldsA = lds; char* ldsB = lds + 16384;
    f32x16 acc[2][2];
#pragma unroll
    for (int i = 0; i < 2; ++i)
#pragma unroll
        for (int j = 0; j < 2; ++j) acc[i][j] = f32x16{};
    const int wm = wid >> 1, wn = wid & 1, h = lane >> 5, l31 = lane & 31;
    if (colt >= 8 && colt < 12) {
        const int n0 = colt * 128;
        gemm_loop<128, 128, 2, 2, false, 2>(p.x, DM, p.wqkv, 3 * DM, DM, m0, n0, 3 * DM, ldsA, ldsB, acc);
#pragma unroll
        for (int mi = 0; mi < 2; ++mi)
#pragma unroll
            for (int ni = 0; ni < 2; ++ni) {
                const int n = n0 - 1024 + wn * 64 + ni * 32 + l31;
                const int head = n >> 4, d = n & 15;
                const int t = (m0 + wm * 64 + mi * 32) >> 5;
#pragma unroll
                for (int s = 0; s < 2; ++s) {
                    u32x4 v;
#pragma unroll
                    for (int j = 0; j < 4; ++j) v[j] = cvtpk(acc[mi][ni][8 * s + 2 * j], acc[mi][ni][8 * s + 2 * j + 1]);
                    *(u32x4*)(p.Vimg + (size_t)head * 32768 + t * 512 + s * 256 + h * 128 + d * 8) = v;
                }
            }
    } else {
        const bool isf = colt >= 12;
        const int n0 = isf ? (colt - 12) * 128 : colt * 128;
        if (isf) gemm_loop<128, 128, 2, 2, true, 2>(p.x, DM, p.w1, DFF, DM, m0, n0, DFF, ldsA, ldsB, acc);
        else     gemm_loop<128, 128, 2, 2, true, 2>(p.x, DM, p.wqkv, 3 * DM, DM, m0, n0, 3 * DM, ldsA, ldsB, acc);
#pragma unroll
        for (int mi = 0; mi < 2; ++mi)
#pragma unroll
            for (int ni = 0; ni < 2; ++ni) {
                const int tok = m0 + wm * 64 + mi * 32 + l31;
#pragma unroll
                for (int g = 0; g < 4; ++g) {
                    const int n = n0 + wn * 64 + ni * 32 + 8 * g + 4 * h;
                    float v0 = acc[mi][ni][4 * g], v1 = acc[mi][ni][4 * g + 1], v2 = acc[mi][ni][4 * g + 2], v3 = acc[mi][ni][4 * g + 3];
                    if (isf) {
                        if (n < DFF) {
                            u32x2 o; o[0] = cvtpk(fmaxf(v0, 0.f), fmaxf(v1, 0.f)); o[1] = cvtpk(fmaxf(v2, 0.f), fmaxf(v3, 0.f));
                            *(u32x2*)(p.Hb + (size_t)tok * DFF + n) = o;
                        }
                    } else if (colt < 4) {
                        u32x2 o; o[0] = cvtpk(v0 * QSCALE, v1 * QSCALE); o[1] = cvtpk(v2 * QSCALE, v3 * QSCALE);
                        *(u32x2*)(p.Q + ((size_t)(n >> 4) * SEQ + tok) * 16 + (n & 15)) = o;
                    } else {
                        const int nn = n - 512;
                        u32x2 o; o[0] = cvtpk(v0, v1); o[1] = cvtpk(v2, v3);
                        *(u32x2*)(p.Kimg + ((size_t)(nn >> 4) * SEQ + tok) * 16 + (nn & 15)) = o;
                    }
                }
            }
    }
}

struct P2 { const u16* Q; const u16* Kimg; const u16* Vimg; u16* O; };
constexpr int A_LDS_K = 0, A_LDS_V = 65536, A_LDS_C = 131072, A_LDS_BYTES = 131072 + 1024;

__global__ void __launch_bounds__(512, 2) k2_attn(P2 p) {
    __shared__ __attribute__((aligned(16))) char lds[A_LDS_BYTES];
    const int b = blockIdx.x, tid = threadIdx.x, lane = tid & 63, wid = tid >> 6;
    const int xcd = b & 7, idx = b >> 3;
    const int head = xcd * 4 + (idx >> 3), qb = idx & 7;
    const int h = lane >> 5, l31 = lane & 31;
    {
        const u16* kg = p.Kimg + (size_t)head * 32768;
        const u16* vg = p.Vimg + (size_t)head * 32768;
#pragma unroll
        for (int i = 0; i < 8; ++i) {
            const int c = tid + 512 * i;
            *(u32x4*)(lds + A_LDS_K + c * 16) = *(const u32x4*)(kg + c * 8);
        }
#pragma unroll
        for (int i = 0; i < 8; ++i) {
            const int c = tid + 512 * i;
            *(u32x4*)(lds + A_LDS_V + c * 16) = *(const u32x4*)(vg + c * 8);
        }
        if (tid < 64) {
            const unsigned v = ((tid & 31) == 0) ? 0x3F803F80u : 0u;
            const u32x4 vv = {v, v, v, v};
            *(u32x4*)(lds + A_LDS_C + (tid & 31) * 16 + (tid >> 5) * 512) = vv;
        }
    }
    const int q0 = qb * 256 + wid * 32;
    const bf16x8 qf = *(const bf16x8*)(p.Q + ((size_t)head * SEQ + q0 + l31) * 16 + h * 8);
    __syncthreads();
    const char* kp = lds + A_LDS_K + l31 * 32 + h * 16;
    const char* vp = lds + ((l31 < 16) ? (A_LDS_V + h * 256 + l31 * 16) : (A_LDS_C + ((l31 == 16) ? 0 : 16)));
    const int vstep = (l31 < 16) ? 1024 : 0;
    f32x16 negm, o = f32x16{};
    {
        const bf16x8 kf = *(const bf16x8*)(kp);
        f32x16 s = MFMA32(kf, qf, f32x16{});
        float mx = s[0];
#pragma unroll
        for (int r = 1; r < 16; ++r) mx = fmaxf(mx, s[r]);
        mx = fmaxf(mx, __shfl_xor(mx, 32));
#pragma unroll
        for (int r = 0; r < 16; ++r) negm[r] = -mx;
    }
    for (int t = 0; t < 64; ++t) {
        const bf16x8 kf = *(const bf16x8*)(kp + t * 1024);
        const bf16x8 v0 = *(const bf16x8*)(vp);
        const bf16x8 v1 = *(const bf16x8*)(vp + 512);
        vp += vstep;
        f32x16 s = MFMA32(kf, qf, negm);
        float mx = fmaxf(s[0], s[1]);
#pragma unroll
        for (int r = 2; r < 16; r += 2) mx = fmaxf(fmaxf(mx, s[r]), s[r + 1]);
        if (__any(mx > RESC_THR)) {
            mx = fmaxf(mx, __shfl_xor(mx, 32));
            const float dl = fmaxf(mx, 0.f);
            const float al = __builtin_amdgcn_exp2f(-dl);
#pragma unroll
            for (int r = 0; r < 16; ++r) { negm[r] -= dl; s[r] -= dl; o[r] *= al; }
        }
        u32x4 p0, p1;
#pragma unroll
        for (int j = 0; j < 4; ++j) {
            p0[j] = cvtpk(__builtin_amdgcn_exp2f(s[2 * j]), __builtin_amdgcn_exp2f(s[2 * j + 1]));
            p1[j] = cvtpk(__builtin_amdgcn_exp2f(s[8 + 2 * j]), __builtin_amdgcn_exp2f(s[8 + 2 * j + 1]));
        }
        o = MFMA32(v0, __builtin_bit_cast(bf16x8, p0), o);
        o = MFMA32(v1, __builtin_bit_cast(bf16x8, p1), o);
    }
    const float lsum = __shfl(o[8], l31);
    const float inv = 1.0f / lsum;
    u16* op = p.O + (size_t)(q0 + l31) * DM + head * 16 + 4 * h;
    u32x2 w0, w1;
    w0[0] = cvtpk(o[0] * inv, o[1] * inv); w0[1] = cvtpk(o[2] * inv, o[3] * inv);
    w1[0] = cvtpk(o[4] * inv, o[5] * inv); w1[1] = cvtpk(o[6] * inv, o[7] * inv);
    *(u32x2*)(op) = w0;
    *(u32x2*)(op + 8) = w1;
}

struct P3 { const u16* O; const u16* wo; const u16* Hb; const u16* w2; const float* G; u16* out; };

__global__ void __launch_bounds__(256, 1) k3_out(P3 p) {
    __shared__ __attribute__((aligned(16))) char lds[65536];
    const int b = blockIdx.x, tid = threadIdx.x, lane = tid & 63, wid = tid >> 6;
    const int colt = b & 7, rowt = b >> 3;
    const int m0 = rowt * 64, n0 = colt * 64;
    char* ldsA = lds; char* ldsB = lds + 8192;
    f32x16 acca[1][1], accb[1][1];
    acca[0][0] = f32x16{}; accb[0][0] = f32x16{};
    gemm_loop<64, 64, 2, 2, true, 4>(p.O, DM, p.wo, DM, DM, m0, n0, DM, ldsA, ldsB, acca);
    gemm_loop<64, 64, 2, 2, true, 4>(p.Hb, DFF, p.w2, DM, DFF, m0, n0, DM, ldsA, ldsB, accb);
    const int wm = wid >> 1, wn = wid & 1, h = lane >> 5, l31 = lane & 31;
    const int tok = m0 + wm * 32 + l31;
    const float g = p.G[tok];
    const float omg = rbf(1.0f - g);
#pragma unroll
    for (int gi = 0; gi < 4; ++gi) {
        const int n = n0 + wn * 32 + 8 * gi + 4 * h;
        float r[4];
#pragma unroll
        for (int j = 0; j < 4; ++j) {
            const float a = rbf(acca[0][0][4 * gi + j]), bb = rbf(accb[0][0][4 * gi + j]);
            r[j] = rbf(g * a) + rbf(omg * bb);
        }
        u32x2 o; o[0] = cvtpk(r[0], r[1]); o[1] = cvtpk(r[2], r[3]);
        *(u32x2*)(p.out + (size_t)tok * DM + n) = o;
    }
}

extern "C" void kernel_launch(void* const* d_in, const int* in_sizes, int n_in,
                              void* d_out, int out_size, void* d_ws, size_t ws_size,
                              hipStream_t stream) {
    const u16* x = (const u16*)d_in[0];
    const u16* wqkv = (const u16*)d_in[1];
    const u16* wo = (const u16*)d_in[2];
    const u16* w1 = (const u16*)d_in[4];
    const u16* w2 = (const u16*)d_in[5];
    const u16* gate_w = (const u16*)d_in[6];
    char* ws = (char*)d_ws;
    u16* Q = (u16*)(ws);
    u16* Kimg = (u16*)(ws + (2u << 20));
    u16* Vimg = (u16*)(ws + (4u << 20));
    u16* O = (u16*)(ws + (6u << 20));
    u16* Hb = (u16*)(ws + (8u << 20));
    float* G = (float*)(ws + (14u << 20));

    P1 p1{x, wqkv, w1, gate_w, Q, Kimg, Vimg, Hb, G};
    k1_proj<<<K1_GEMM_BLOCKS + K1_GATE_BLOCKS, 256, 0, stream>>>(p1);
    P2 p2{Q, Kimg, Vimg, O};
    k2_attn<<<256, 512, 0, stream>>>(p2);
    P3 p3{O, wo, Hb, w2, G, (u16*)d_out};
    k3_out<<<256, 256, 0, stream>>>(p3);
}
```
